```python
import torch

def _softmax(x, axis=-1):
    return torch.softmax(x, dim=axis)
def _gelu(x):
    return torch.nn.functional.gelu(x, approximate="tanh")
def _silu(x):
    return torch.nn.functional.silu(x)
def _sigmoid(x):
    return torch.sigmoid(x)
def _layernorm(x, axis=-1, eps=1e-5):
    m = x.mean(dim=axis, keepdim=True)
    v = x.var(dim=axis, keepdim=True, unbiased=False)
    return (x - m) / torch.sqrt(v + eps)
def _scatter_add(tgt, idx, src, axis=0):
    return tgt.clone().index_add_(axis, idx, src)
def _segment_sum(src, seg, n):
    out = torch.zeros((n,) + src.shape[1:], dtype=src.dtype)
    return out.index_add_(0, seg, src)
def _pad(x, pw):
    flat = tuple(v for pair in reversed(pw) for v in pair)
    return torch.nn.functional.pad(x, flat)

kernel_name = 'standalone__paged_block_table_kv_pagedattention__decode'

def setup_inputs():

    _w = (lambda a: (lambda t: t.to(torch.bfloat16) if t.is_floating_point() else t)(torch.as_tensor(a)))
    B, Hq, Hkv, Sq, Skv, D = 1, 32, 32, 1, 4096, 128
    bs = 16
    nbps = max(1, Skv // bs)
    num_phys = B * nbps + 4
    bt = torch.randperm(num_phys)[: B * nbps].reshape(B, nbps).to(torch.int64)
    cl = (nbps * bs - (torch.arange(1, B + 1, dtype=torch.int64) * 2654435761) % max(1, nbps * bs // 2)).to(torch.int64)
    return {
        "q": _w((lambda *s: torch.randn(*s, dtype=torch.bfloat16) * 0.8)(B, Hq, Sq, D)),
        "k_cache": _w((lambda *s: torch.randn(*s, dtype=torch.bfloat16) * 0.8)(num_phys, bs, Hkv, D)),
        "v_cache": _w((lambda *s: torch.randn(*s, dtype=torch.bfloat16) * 0.05)(num_phys, bs, Hkv, D)),
        "block_table": _w(bt),
        "context_lens": _w(cl),
    }

def reference(q, k_cache, v_cache, block_table, context_lens):
    B, Hq, Sq, D = q.shape
    bs = k_cache.shape[1]
    Hkv = k_cache.shape[2]
    nbps = block_table.shape[1]
    Skv = nbps * bs
    bt = block_table.reshape(-1)
    k = (lambda x, *d: x.permute(*d))(k_cache[bt].reshape(B, Skv, Hkv, D), 0, 2, 1, 3)
    v = (lambda x, *d: x.permute(*d))(v_cache[bt].reshape(B, Skv, Hkv, D), 0, 2, 1, 3)
    g = Hq // Hkv
    qg = q.reshape(B, Hkv, g, Sq, D)
    scores = torch.einsum("bhgqd,bhkd->bhgqk", qg, k) * (D ** -0.5)
    pos = torch.arange(Skv)
    msk = pos[None, :] < context_lens[:, None]
    scores = torch.where(msk[:, None, None, None, :], scores, -1e9)
    p = _softmax(scores, axis=-1)
    out = torch.einsum("bhgqk,bhkd->bhgqd", p, v)
    return out.reshape(B, Hq, Sq, D)
```

```cpp
#include <hip/hip_runtime.h>
#include <hip/hip_bf16.h>
#include <stdint.h>

#define KV_NT 0

typedef unsigned u32x4 __attribute__((ext_vector_type(4)));
typedef float f32x4 __attribute__((ext_vector_type(4)));
typedef __bf16 bf16x2 __attribute__((ext_vector_type(2)));
typedef __bf16 bf16x8 __attribute__((ext_vector_type(8)));
#define GAS __attribute__((address_space(1)))
typedef GAS unsigned gu32;
typedef GAS unsigned long long gu64;
typedef GAS u32x4 gv4;
#define RLX_AGENT __ATOMIC_RELAXED, __HIP_MEMORY_SCOPE_AGENT

constexpr int H = 32, D = 128, PAGE = 16, NBPS = 256, SKV = NBPS * PAGE;
constexpr int NSPLIT = 8, WAVES = 16, NTHR = WAVES * 64;
constexpr int SLOTS = NSPLIT * WAVES;
constexpr unsigned TOK_BYTES = H * D * 2;
constexpr unsigned PAGE_BYTES = PAGE * TOK_BYTES;
constexpr float C2 = 0.088388347648318440f * 1.4426950408889634f;
constexpr float MASK_L2 = -998244352.0f * 1.4426950408889634f;
constexpr size_t WS_EPOCH = 0;
constexpr size_t WS_ML = 4096;
constexpr size_t WS_O = 8192;
constexpr int PSTR = 136;

template <int CTRL> __device__ __forceinline__ float dpp(float x) {
  return __builtin_bit_cast(float, __builtin_amdgcn_mov_dpp(__builtin_bit_cast(int, x), CTRL, 0xf, 0xf, true));
}
constexpr int XOR1 = 0xB1, XOR2 = 0x4E, HMIR = 0x141, MIR = 0x140, ROR8 = 0x128;
__device__ __forceinline__ float row_sum16(float x) { x += dpp<XOR1>(x); x += dpp<XOR2>(x); x += dpp<HMIR>(x); x += dpp<MIR>(x); return x; }
__device__ __forceinline__ float row_max16(float x) { x = fmaxf(x, dpp<XOR1>(x)); x = fmaxf(x, dpp<XOR2>(x)); x = fmaxf(x, dpp<HMIR>(x)); x = fmaxf(x, dpp<MIR>(x)); return x; }
__device__ __forceinline__ float xrow_max(float x) {
  auto s = __builtin_amdgcn_permlane16_swap(__float_as_uint(x), __float_as_uint(x), false, false);
  x = fmaxf(__uint_as_float(s[0]), __uint_as_float(s[1]));
  auto t = __builtin_amdgcn_permlane32_swap(__float_as_uint(x), __float_as_uint(x), false, false);
  return fmaxf(__uint_as_float(t[0]), __uint_as_float(t[1]));
}
__device__ __forceinline__ float xrow_sum(float x) {
  auto s = __builtin_amdgcn_permlane16_swap(__float_as_uint(x), __float_as_uint(x), false, false);
  x = __uint_as_float(s[0]) + __uint_as_float(s[1]);
  auto t = __builtin_amdgcn_permlane32_swap(__float_as_uint(x), __float_as_uint(x), false, false);
  return __uint_as_float(t[0]) + __uint_as_float(t[1]);
}
__device__ __forceinline__ float dot2(unsigned k, unsigned q, float acc) {
  return __builtin_amdgcn_fdot2_f32_bf16(__builtin_bit_cast(bf16x2, k), __builtin_bit_cast(bf16x2, q), acc, false);
}
__device__ __forceinline__ u32x4 ldkv(const char* p) {
  return *(const gv4*)p;
}
__device__ __forceinline__ unsigned bf16_bits(float x) {
  return (unsigned)__builtin_bit_cast(unsigned short, (__bf16)x);
}

struct Smem {
  float part[WAVES * PSTR];
  float smax[WAVES];
  float ssum[WAVES];
  unsigned tag;
  unsigned pad[3];
};

template <int NP>
__device__ __forceinline__ float wave_body(Smem& sm, const char* kp0, const char* kp1, const char* vp0, const char* vp1,
                                           u32x4 q, int rem0, int rem1, int wave, int lane) {
  const int r = lane >> 4, c = lane & 15;
  constexpr int NL = NP * 4;
  u32x4 K[NL > 0 ? NL : 1], V[NL > 0 ? NL : 1];
#pragma unroll
  for (int i = 0; i < NL; ++i) K[i] = ldkv((i < 4 ? kp0 : kp1) + (size_t)(i & 3) * 4 * TOK_BYTES);
#pragma unroll
  for (int i = 0; i < NL; ++i) V[i] = ldkv((i < 4 ? vp0 : vp1) + (size_t)(i & 3) * 4 * TOK_BYTES);
  float s[NL > 0 ? NL : 1];
  float m = -INFINITY;
#pragma unroll
  for (int i = 0; i < NL; ++i) {
    float x = dot2(K[i][0], q[0], 0.f); x = dot2(K[i][1], q[1], x); x = dot2(K[i][2], q[2], x); x = dot2(K[i][3], q[3], x);
    x = row_sum16(x);
    const bool live = (4 * (i & 3) + r) < (i < 4 ? rem0 : rem1);
    s[i] = live ? x * C2 : -INFINITY;
    m = fmaxf(m, s[i]);
  }
  if (NL > 0) m = xrow_max(m);
  if (lane == 0) sm.smax[wave] = m;
  __builtin_amdgcn_s_waitcnt(0xC07F);
  __builtin_amdgcn_s_barrier();
  const float M = row_max16(sm.smax[c]);
  const float msub = (M == -INFINITY) ? 0.f : M;
  f32x4 acc = {0.f, 0.f, 0.f, 0.f};
  float l = 0.f;
  if (NL > 0) {
    const unsigned sel = (c & 1) ? 0xffff0000u : 0x0000ffffu;
    const int dw = (c >> 1) & 3;
    const unsigned mk0 = dw == 0 ? sel : 0u, mk1 = dw == 1 ? sel : 0u, mk2 = dw == 2 ? sel : 0u, mk3 = dw == 3 ? sel : 0u;
    u32x4 B[NL > 0 ? NL : 1];
#pragma unroll
    for (int i = 0; i < NL; ++i) {
      const float p = __builtin_amdgcn_exp2f(s[i] - msub);
      l += p;
      const unsigned hi = bf16_bits(p);
      const float rest = p - __uint_as_float(hi << 16);
      const unsigned lo = bf16_bits(rest);
      const unsigned v = c < 8 ? hi : lo;
      const unsigned dup = v | (v << 16);
      B[i] = u32x4{dup & mk0, dup & mk1, dup & mk2, dup & mk3};
    }
    l = xrow_sum(l);
#pragma unroll
    for (int i = 0; i < NL; ++i)
      acc = __builtin_amdgcn_mfma_f32_16x16x32_bf16(__builtin_bit_cast(bf16x8, V[i]), __builtin_bit_cast(bf16x8, B[i]), acc, 0, 0, 0);
#pragma unroll
    for (int t = 0; t < 4; ++t) acc[t] += dpp<ROR8>(acc[t]);
  }
  if (lane == 0) sm.ssum[wave] = l;
  if (c < 8) *(f32x4*)&sm.part[wave * PSTR + (r * 8 + c) * 4] = acc;
  return M;
}

__global__ void __launch_bounds__(NTHR) paged_decode_kernel(const char* __restrict__ qp, const char* __restrict__ kc, const char* __restrict__ vc,
                                                          const int* __restrict__ bt, const int* __restrict__ cl,
                                                          unsigned short* __restrict__ out, char* __restrict__ ws) {
  __shared__ __attribute__((aligned(16))) Smem sm;
  const int split = blockIdx.x & (NSPLIT - 1), h = blockIdx.x >> 3;
  const int tid = threadIdx.x, lane = tid & 63, wave = __builtin_amdgcn_readfirstlane(tid >> 6);
  const int r = lane >> 4, c = lane & 15;
  const int lp0 = wave * NSPLIT + split, lp1 = lp0 + SLOTS;
  const int ph0 = bt[lp0], ph1 = bt[lp1];
  const int len_raw = cl[0];
  const u32x4 q = *(const gv4*)(qp + h * (D * 2) + c * 16);
  gu32* epoch = (gu32*)(ws + WS_EPOCH + h * 128);
  if (tid == 0) sm.tag = __hip_atomic_load(epoch, RLX_AGENT) + 1u;
  const int len = len_raw < 0 ? 0 : (len_raw > SKV ? SKV : len_raw);
  const int rem0 = len - lp0 * PAGE, rem1 = len - lp1 * PAGE;
  const size_t loff = (size_t)h * (D * 2) + (size_t)r * TOK_BYTES + c * 16;
  const char* kp0 = kc + (size_t)(unsigned)ph0 * PAGE_BYTES + loff;
  const char* kp1 = kc + (size_t)(unsigned)ph1 * PAGE_BYTES + loff;
  const char* vp0 = vc + (size_t)(unsigned)ph0 * PAGE_BYTES + loff;
  const char* vp1 = vc + (size_t)(unsigned)ph1 * PAGE_BYTES + loff;
  float M;
  if (rem1 > 0) M = wave_body<2>(sm, kp0, kp1, vp0, vp1, q, rem0, rem1, wave, lane);
  else if (rem0 > 0) M = wave_body<1>(sm, kp0, kp1, vp0, vp1, q, rem0, rem1, wave, lane);
  else M = wave_body<0>(sm, kp0, kp1, vp0, vp1, q, rem0, rem1, wave, lane);
  __builtin_amdgcn_s_waitcnt(0xC07F);
  __builtin_amdgcn_s_barrier();
  const int g = lane >> 3, d = lane & 7;
  const float own = sm.part[g * PSTR + 8 * wave + d] + sm.part[(g + 8) * PSTR + 8 * wave + d];
  const float L = row_sum16(sm.ssum[c]);
  const unsigned tag = sm.tag;
  gu64* gO = (gu64*)(ws + WS_O) + (size_t)h * NSPLIT * D;
  gu64* gML = (gu64*)(ws + WS_ML) + (size_t)h * NSPLIT * 2;
  if (split != 0) {
    float x = own; x += dpp<ROR8>(x); x = xrow_sum(x);
    if (lane < 8) __hip_atomic_store(gO + split * D + 8 * wave + d, ((unsigned long long)tag << 32) | __float_as_uint(x), RLX_AGENT);
    if (wave == 0 && lane >= 8 && lane < 10)
      __hip_atomic_store(gML + split * 2 + (lane - 8), ((unsigned long long)tag << 32) | __float_as_uint(lane == 8 ? M : L), RLX_AGENT);
    return;
  }
  float og = 0.f, mg = M, lg = L;
  for (unsigned spins = 0; spins < (1u << 22); ++spins) {
    bool ok = true;
    if (g != 0) {
      const unsigned long long a = __hip_atomic_load(gO + g * D + 8 * wave + d, RLX_AGENT);
      const unsigned long long b0 = __hip_atomic_load(gML + g * 2, RLX_AGENT);
      const unsigned long long b1 = __hip_atomic_load(gML + g * 2 + 1, RLX_AGENT);
      og = __uint_as_float((unsigned)a); mg = __uint_as_float((unsigned)b0); lg = __uint_as_float((unsigned)b1);
      ok = (unsigned)(a >> 32) == tag && (unsigned)(b0 >> 32) == tag && (unsigned)(b1 >> 32) == tag;
    }
    if (__all(ok)) break;
  }
  if (tid == 0) __hip_atomic_store(epoch, tag, RLX_AGENT);
  float Mx = fmaxf(mg, dpp<ROR8>(mg)); Mx = xrow_max(Mx);
  const bool slow = (len < SKV) && !(MASK_L2 - Mx < -160.f);
  if (slow) Mx = fmaxf(Mx, MASK_L2);
  const float msub = (Mx == -INFINITY) ? 0.f : Mx;
  const float wg = __builtin_amdgcn_exp2f(mg - msub), w0 = __builtin_amdgcn_exp2f(M - msub);
  float x = w0 * own + wg * og;
  float y = wg * lg;
  const int dim = 32 * (wave >> 2) + 8 * (d & 3) + 2 * (wave & 3) + (d >> 2);
  if (slow) {
    const float wm = __builtin_amdgcn_exp2f(MASK_L2 - msub);
    float sv = 0.f, cnt = 0.f;
    for (int t = len + g; t < SKV; t += 8) {
      const unsigned short* vr = (const unsigned short*)(vc + (size_t)(unsigned)bt[t >> 4] * PAGE_BYTES + (size_t)(t & 15) * TOK_BYTES + h * (D * 2));
      sv += __uint_as_float((unsigned)vr[dim] << 16); cnt += 1.f;
    }
    x += wm * sv; y += wm * cnt;
  }
  x += dpp<ROR8>(x); x = xrow_sum(x);
  y += dpp<ROR8>(y); y = xrow_sum(y);
  if (lane < 8) out[h * D + dim] = (unsigned short)bf16_bits(x / y);
}

extern "C" void kernel_launch(void* const* d_in, const int* in_sizes, int n_in, void* d_out, int out_size, void* d_ws, size_t ws_size,
                              hipStream_t stream) {
  paged_decode_kernel<<<dim3(H * NSPLIT), dim3(NTHR), 0, stream>>>((const char*)d_in[0], (const char*)d_in[1], (const char*)d_in[2],
                                                                  (const int*)d_in[3], (const int*)d_in[4], (unsigned short*)d_out, (char*)d_ws);
}
```

```cpp
#include <hip/hip_runtime.h>
#include <hip/hip_bf16.h>
#include <stdint.h>

#define WP 4
#define PF_TLB 2
#define PF_CODE 0
#define EPE 1
#define OUTM 0
#define MLM 1
#define PIPE 1
#define TH 8
#define SLP 8
#define FLAG2 2

typedef unsigned u32x4 __attribute__((ext_vector_type(4)));
typedef float f32x4 __attribute__((ext_vector_type(4)));
typedef __bf16 bf16x2 __attribute__((ext_vector_type(2)));
typedef __bf16 bf16x8 __attribute__((ext_vector_type(8)));
#define GAS __attribute__((address_space(1)))
typedef GAS unsigned gu32;
typedef GAS u32x4 gv4;
typedef __attribute__((address_space(4))) const int cint;
#define RLX_AGENT __ATOMIC_RELAXED, __HIP_MEMORY_SCOPE_AGENT

constexpr int H = 32, D = 128, PAGE = 16, NBPS = 256, SKV = NBPS * PAGE;
constexpr int NSPLIT = 32, WAVES = 4, NTHR = (WAVES + 1) * 64, NG = 8;
constexpr unsigned TOK_BYTES = H * D * 2;
constexpr unsigned PAGE_BYTES = PAGE * TOK_BYTES;
constexpr float C2 = 0.088388347648318440f * 1.4426950408889634f;
constexpr float MASK_L2 = -998244352.0f * 1.4426950408889634f;
constexpr unsigned WS_EPOCH = 0;
constexpr unsigned WS_ML = 4096;
constexpr unsigned WS_O = 32768;
constexpr unsigned WS_BYTES = WS_O + H * NSPLIT * 1024;
constexpr int AUX_NT = 2, AUX_SC1 = 16;

template <int CTRL> __device__ __forceinline__ float dpp(float x) {
  return __builtin_bit_cast(float, __builtin_amdgcn_mov_dpp(__builtin_bit_cast(int, x), CTRL, 0xf, 0xf, true));
}
template <int CTRL> __device__ __forceinline__ unsigned dppu(unsigned x) { return (unsigned)__builtin_amdgcn_mov_dpp((int)x, CTRL, 0xf, 0xf, true); }
constexpr int XOR1 = 0xB1, XOR2 = 0x4E, ROR4 = 0x124, ROR8 = 0x128, BCAST = 0x150;
__device__ __forceinline__ float fmx(float a, float b) { return __builtin_fmaxf(a, b); }
__device__ __forceinline__ float xrow_max(float x) {
  auto s = __builtin_amdgcn_permlane16_swap(__float_as_uint(x), __float_as_uint(x), false, false);
  x = fmx(__uint_as_float(s[0]), __uint_as_float(s[1]));
  auto t = __builtin_amdgcn_permlane32_swap(__float_as_uint(x), __float_as_uint(x), false, false);
  return fmx(__uint_as_float(t[0]), __uint_as_float(t[1]));
}
__device__ __forceinline__ float xrow_sum(float x) {
  auto s = __builtin_amdgcn_permlane16_swap(__float_as_uint(x), __float_as_uint(x), false, false);
  x = __uint_as_float(s[0]) + __uint_as_float(s[1]);
  auto t = __builtin_amdgcn_permlane32_swap(__float_as_uint(x), __float_as_uint(x), false, false);
  return __uint_as_float(t[0]) + __uint_as_float(t[1]);
}
__device__ __forceinline__ float dot2(unsigned k, unsigned q, float acc) {
  return __builtin_amdgcn_fdot2_f32_bf16(__builtin_bit_cast(bf16x2, k), __builtin_bit_cast(bf16x2, q), acc, false);
}
__device__ __forceinline__ unsigned cvtpk(float lo, float hi) {
  bf16x2 v = {(__bf16)lo, (__bf16)hi}; return __builtin_bit_cast(unsigned, v);
}
__device__ __forceinline__ float red4(float a, float b, float c, float d) {
  float u, w, s;
  asm("s_nop 2\n\t"
      "v_add_f32_dpp %0, %3, %3 row_ror:8 row_mask:0xf bank_mask:0x3\n\t"
      "v_add_f32_dpp %1, %4, %4 row_ror:8 row_mask:0xf bank_mask:0x3\n\t"
      "v_add_f32_dpp %0, %5, %5 row_ror:8 row_mask:0xf bank_mask:0xc\n\t"
      "v_add_f32_dpp %1, %6, %6 row_ror:8 row_mask:0xf bank_mask:0xc\n\t"
      "s_nop 1\n\t"
      "v_add_f32_dpp %2, %0, %0 row_half_mirror row_mask:0xf bank_mask:0x5\n\t"
      "v_add_f32_dpp %2, %1, %1 row_half_mirror row_mask:0xf bank_mask:0xa\n\t"
      "s_nop 1\n\t"
      "v_add_f32_dpp %2, %2, %2 quad_perm:[1,0,3,2] row_mask:0xf bank_mask:0xf\n\t"
      "s_nop 1\n\t"
      "v_add_f32_dpp %2, %2, %2 quad_perm:[2,3,0,1] row_mask:0xf bank_mask:0xf\n\t"
      "s_nop 1"
      : "=&v"(u), "=&v"(w), "=&v"(s) : "v"(a), "v"(b), "v"(c), "v"(d));
  return s;
}

#define ST(k)
#define WRST()
#define SB() __builtin_amdgcn_sched_barrier(0)

struct Args { const char* q; const char* k; const char* v; const int* bt; const int* cl; unsigned* out; char* ws; unsigned long long kv_bytes; };

__global__ void __launch_bounds__(NTHR, 2) paged_decode_kernel(Args a) {
  __shared__ unsigned long long flags[2];
  const int lane = threadIdx.x & 63, wave = __builtin_amdgcn_readfirstlane(threadIdx.x >> 6);
  const int j = blockIdx.x & (NSPLIT - 1), h = (blockIdx.x >> 5) * WAVES + (wave < WAVES ? wave : (j & 3));
  gu32* epoch = (gu32*)(a.ws + WS_EPOCH + h * 128);
  const auto rw = __builtin_amdgcn_make_buffer_rsrc((void*)a.ws, 0, WS_BYTES, 0x00020000);
  const int half = (j >> 3) & 1, pres = (j & 7) + 8 * (j >> 4);
  int ph[2 * NG];
#pragma unroll
  for (int u = 0; u < 2 * NG; ++u) ph[u] = ((cint*)a.bt)[pres + 16 * u];
  const int len_raw = *(cint*)a.cl;
  asm volatile("" :: "s"(a.q), "s"(a.k), "s"(a.v), "s"(a.out), "s"(a.ws), "s"(a.kv_bytes));
  if (__builtin_expect(wave < WAVES, 1)) {
    const int r = lane >> 4, c = lane & 15;
    const u32x4 q = *(const gv4*)(a.q + h * (D * 2) + c * 16);
    const unsigned ep = __hip_atomic_load(epoch, RLX_AGENT);
    const int rem0 = len_raw - (pres * PAGE + half * 8);
    const unsigned voff0 = (unsigned)h * (D * 2) + (unsigned)(half * 8 + r) * TOK_BYTES + c * 16, voff1 = voff0 + 4 * TOK_BYTES;
    const auto rk = __builtin_amdgcn_make_buffer_rsrc((void*)a.k, 0, (unsigned)a.kv_bytes, 0x00020000);
    const auto rv = __builtin_amdgcn_make_buffer_rsrc((void*)a.v, 0, (unsigned)a.kv_bytes, 0x00020000);
    u32x4 buf[2 * NG][4];
    unsigned so[2 * NG];
    auto issue = [&](int n) {
      const int t = n & (NG - 1);
#pragma unroll
      for (int b = 0; b < 4; ++b) {
        const int u = 2 * t + (b >> 1);
        if (n < NG && (b & 1) == 0) so[u] = u * 16 * PAGE < rem0 ? (unsigned)ph[u] * PAGE_BYTES : 0x80000000u;
        buf[n][b] = __builtin_amdgcn_raw_buffer_load_b128(n < NG ? rk : rv, (b & 1) ? voff1 : voff0, so[u], AUX_NT);
      }
    };
#pragma unroll
    for (int n = 0; n < WP; ++n) { issue(n); SB(); }
    __builtin_amdgcn_s_barrier();
    SB();
    ST(2);
    const int remv = rem0 - (256 * (c >> 3) + (c & 4) + r);
    float S[NG];
    float m = -INFINITY;
#pragma unroll
    for (int t = 0; t < NG; ++t) {
      float x[4];
#pragma unroll
      for (int i = 0; i < 4; ++i) {
        x[i] = dot2(buf[t][i][0], q[0], 0.f); x[i] = dot2(buf[t][i][1], q[1], x[i]); x[i] = dot2(buf[t][i][2], q[2], x[i]); x[i] = dot2(buf[t][i][3], q[3], x[i]);
      }
      SB();
      if (t + WP < 2 * NG) issue(t + WP);
      SB();
      const float s = red4(x[0], x[1], x[2], x[3]);
      S[t] = 512 * t < remv ? s : -INFINITY;
      m = fmx(m, S[t]);
    }
    ST(4);
    m = fmx(m, dpp<ROR4>(m)); m = fmx(m, dpp<ROR8>(m)); m = xrow_max(m);
    const float mc = m * C2;
    const float msub = (m == -INFINITY) ? 0.f : -mc;
    unsigned P[NG];
    float l = 0.f;
#pragma unroll
    for (int t = 0; t < NG; ++t) {
      const float p = __builtin_amdgcn_exp2f(__builtin_fmaf(S[t], C2, msub));
      P[t] = cvtpk(p, p);
      l += __uint_as_float(P[t] & 0xffff0000u);
    }
    l += dpp<ROR4>(l); l += dpp<ROR8>(l); l = xrow_sum(l);
    const unsigned tag = ep + 1u ? ep + 1u : 1u;
    const bool sig = (j & 3) == wave && lane == 0;
    if (lane == 0) __builtin_amdgcn_raw_buffer_store_b128(u32x4{__float_as_uint(mc), tag, __float_as_uint(l), tag}, rw, 0, WS_ML + (h * NSPLIT + j) * 16, AUX_SC1);
    if (sig) __hip_atomic_store(&flags[0], (1ull << 32) | tag, __ATOMIC_RELAXED, __HIP_MEMORY_SCOPE_WORKGROUP);
    const unsigned sel = (c & 1) ? 0xffff0000u : 0x0000ffffu;
    const int dw = (c >> 1) & 3;
    const unsigned mk0 = dw == 0 ? sel : 0u, mk1 = dw == 1 ? sel : 0u, mk2 = dw == 2 ? sel : 0u, mk3 = dw == 3 ? sel : 0u;
    f32x4 acc = {0.f, 0.f, 0.f, 0.f};
    ST(5);
#pragma unroll
    for (int t = 0; t < NG; ++t) {
      const unsigned b0 = dppu<BCAST + 0>(P[t]), b1 = dppu<BCAST + 4>(P[t]), b2 = dppu<BCAST + 8>(P[t]), b3 = dppu<BCAST + 12>(P[t]);
      const unsigned bb[4] = {b0, b1, b2, b3};
#pragma unroll
      for (int i = 0; i < 4; ++i) {
        const u32x4 A = {bb[i] & mk0, bb[i] & mk1, bb[i] & mk2, bb[i] & mk3};
        acc = __builtin_amdgcn_mfma_f32_16x16x32_bf16(__builtin_bit_cast(bf16x8, A), __builtin_bit_cast(bf16x8, buf[NG + t][i]), acc, 0, 0, 0);
      }
      SB();
      if (NG + t + WP < 2 * NG) issue(NG + t + WP);
      if (t == FLAG2 && sig) __hip_atomic_store(&flags[1], 1ull, __ATOMIC_RELAXED, __HIP_MEMORY_SCOPE_WORKGROUP);
      SB();
    }
    ST(6);
    const unsigned v0 = __float_as_uint(r < 2 ? acc[0] : acc[2]), v1 = __float_as_uint(r < 2 ? acc[1] : acc[3]);
    __builtin_amdgcn_raw_buffer_store_b128(u32x4{v0, tag, v1, tag}, rw, lane * 16, WS_O + (h * NSPLIT + j) * 1024, AUX_SC1);
    ST(7);
    WRST();
    return;
  }
  {
    if (lane < 2) __hip_atomic_store(&flags[lane], 0ull, __ATOMIC_RELAXED, __HIP_MEMORY_SCOPE_WORKGROUP);
    __builtin_amdgcn_s_waitcnt(0xC07F);
    __builtin_amdgcn_s_barrier();
    {
      unsigned off = (unsigned)(lane & 31) << 21;
      off = off < (unsigned)a.kv_bytes ? off : (unsigned)a.kv_bytes - 4;
      const char* p = (lane < 32 ? a.k : a.v) + off;
      if (lane == 63) p = (const char*)a.out;
      if (PF_TLB == 2) p = (const char*)a.out;
      const unsigned pf1 = *(const gu32*)p;
      asm volatile("" :: "v"(pf1));
    }
    const int len = len_raw < 0 ? 0 : (len_raw > SKV ? SKV : len_raw);
    const int dp = 4 * (8 * ((j >> 2) & 1) + (lane & 7)) + 2 * ((j >> 3) & 1) + (j >> 4);
    ST(1); ST(2);
    unsigned long long f0;
    for (;;) {
      f0 = __hip_atomic_load(&flags[0], __ATOMIC_RELAXED, __HIP_MEMORY_SCOPE_WORKGROUP);
      if (f0 >> 32) break;
      __builtin_amdgcn_s_sleep(8);
    }
    const unsigned tag = (unsigned)f0;
    ST(3);
    u32x4 ml[4];
    unsigned vo[4], o0[4] = {0u, 0u, 0u, 0u}, o1[4] = {0u, 0u, 0u, 0u}, miss;
#pragma unroll
    for (int k = 0; k < 4; ++k) vo[k] = (k * 8 + (lane >> 3)) * 1024 + (lane & 7) * 16;
    const unsigned so = WS_O + h * NSPLIT * 1024 + (j >> 2) * 128;
    auto take = [&](const u32x4* g) {
      miss = 0u;
#pragma unroll
      for (int k = 0; k < 4; ++k) {
        const bool o = g[k][1] == tag && g[k][3] == tag;
        o0[k] = o ? g[k][0] : o0[k]; o1[k] = o ? g[k][2] : o1[k];
        vo[k] = o ? 0x80000000u : vo[k];
        miss += (~vo[k]) >> 31;
      }
    };
    while (__hip_atomic_load(&flags[1], __ATOMIC_RELAXED, __HIP_MEMORY_SCOPE_WORKGROUP) == 0ull) __builtin_amdgcn_s_sleep(2);
    for (unsigned spins = 0u; spins < (1u << 22); ++spins) {
      bool o = true;
#pragma unroll
      for (int k = 0; k < 4; ++k) ml[k] = __builtin_amdgcn_raw_buffer_load_b128(rw, (lane >> 3) * 16 + k * 128, WS_ML + h * NSPLIT * 16, AUX_SC1);
#pragma unroll
      for (int k = 0; k < 4; ++k) o = o && ml[k][1] == tag && ml[k][3] == tag;
      asm volatile("" ::: "memory");
      if (__all(o)) break;
    }
    ST(4);
    if (j < 4 && lane == 0) __hip_atomic_store(epoch, tag, RLX_AGENT);
    u32x4 srd = {(unsigned)(size_t)a.ws, (unsigned)((size_t)a.ws >> 32) & 0xffffu, WS_BYTES, 0x00020000u};
    u32x4 A[4], B[4];
#define PLD(X, S) do { _Pragma("unroll") for (int k = 0; k < 4; ++k) asm volatile("buffer_load_dwordx4 %0, %1, %2, %3 offen sc1" : "=v"(X[k]) : "v"(vo[k]), "s"(srd), "s"(S) : "memory"); } while (0)
#define PWAIT(X) asm volatile("s_waitcnt vmcnt(4)" : "+v"(X[0]), "+v"(X[1]), "+v"(X[2]), "+v"(X[3]) :: "memory")
    PLD(A, so);
    float wn[4], e0 = 0.f, e1 = 0.f;
    {
      float Mx = fmx(fmx(__uint_as_float(ml[0][0]), __uint_as_float(ml[1][0])), fmx(__uint_as_float(ml[2][0]), __uint_as_float(ml[3][0])));
      Mx = fmx(Mx, dpp<ROR8>(Mx)); Mx = xrow_max(Mx);
      const bool slow = (len < SKV) && !(MASK_L2 - Mx < -160.f);
      if (slow) Mx = fmx(Mx, MASK_L2);
      const float ms2 = (Mx == -INFINITY) ? 0.f : Mx;
      float y = 0.f;
#pragma unroll
      for (int k = 0; k < 4; ++k) { wn[k] = __builtin_amdgcn_exp2f(__uint_as_float(ml[k][0]) - ms2); y = __builtin_fmaf(wn[k], __uint_as_float(ml[k][2]), y); }
      if (__builtin_expect(slow, 0)) {
        const float wm = __builtin_amdgcn_exp2f(MASK_L2 - ms2);
        float cnt = 0.f;
        for (int t = len + (lane >> 3); t < SKV; t += 8) {
          const unsigned vv = *(const gu32*)(a.v + (size_t)(unsigned)a.bt[t >> 4] * PAGE_BYTES + (size_t)(t & 15) * TOK_BYTES + h * (D * 2) + dp * 4);
          e0 += __uint_as_float(vv << 16); e1 += __uint_as_float(vv & 0xffff0000u); cnt += 1.f;
        }
        e0 *= wm; e1 *= wm; y += wm * cnt;
      }
      y += dpp<ROR8>(y); y = xrow_sum(y);
      const float ry = __builtin_amdgcn_rcpf(y);
#pragma unroll
      for (int k = 0; k < 4; ++k) wn[k] *= ry;
      e0 *= ry; e1 *= ry;
      asm volatile("" : "+v"(wn[0]), "+v"(wn[1]), "+v"(wn[2]), "+v"(wn[3]), "+v"(e0), "+v"(e1));
    }
    ST(5);
    ST(6);
    unsigned sb = 0x80000000u;
    for (unsigned spins = 0u; spins < (1u << 21); ++spins) {
      PLD(B, sb);
      PWAIT(A);
      take(A);
      unsigned long long mm = __ballot(miss != 0u);
      if (mm == 0ull) break;
      PLD(A, so);
      PWAIT(B);
      take(B);
      mm = __ballot(miss != 0u);
      if (mm == 0ull) break;
      if (sb != so && __builtin_popcountll(mm) + __builtin_popcountll(__ballot(miss > 1u)) * 2 <= TH * 8) { sb = so; __builtin_amdgcn_s_sleep(SLP); }
    }
    float x0 = e0, x1 = e1;
#pragma unroll
    for (int k = 0; k < 4; ++k) { x0 = __builtin_fmaf(wn[k], __uint_as_float(o0[k]), x0); x1 = __builtin_fmaf(wn[k], __uint_as_float(o1[k]), x1); }
    ST(7);
    x0 += dpp<ROR8>(x0); x1 += dpp<ROR8>(x1);
    x0 = xrow_sum(x0); x1 = xrow_sum(x1);
    if (lane < 8) a.out[h * (D / 2) + dp] = cvtpk(x0, x1);
    asm volatile("s_waitcnt vmcnt(0)" :: "v"(A[0]), "v"(A[1]), "v"(A[2]), "v"(A[3]), "v"(B[0]), "v"(B[1]), "v"(B[2]), "v"(B[3]));
  }
  WRST();
}

extern "C" void kernel_launch(void* const* d_in, const int* in_sizes, int n_in, void* d_out, int out_size, void* d_ws, size_t ws_size,
                              hipStream_t stream) {
  Args a{(const char*)d_in[0], (const char*)d_in[1], (const char*)d_in[2], (const int*)d_in[3], (const int*)d_in[4],
         (unsigned*)d_out, (char*)d_ws, (unsigned long long)in_sizes[1] * 2ull};
  paged_decode_kernel<<<dim3(H * NSPLIT / WAVES), dim3(NTHR), 0, stream>>>(a);
}
```

```cpp
#include <hip/hip_runtime.h>
#include <hip/hip_bf16.h>
#include <stdint.h>

#define WP 4
#define PF_TLB 2
#define PF_CODE 0
#define EPE 1
#define OUTM 0
#define TIE 5
#define MLM 1
#define PIPE 1
#define TH 8
#define SLP 8
#define FLAG2 2

typedef unsigned u32x4 __attribute__((ext_vector_type(4)));
typedef float f32x4 __attribute__((ext_vector_type(4)));
typedef __bf16 bf16x2 __attribute__((ext_vector_type(2)));
typedef __bf16 bf16x8 __attribute__((ext_vector_type(8)));
#define GAS __attribute__((address_space(1)))
typedef GAS unsigned gu32;
typedef GAS u32x4 gv4;
typedef __attribute__((address_space(4))) const int cint;
#define RLX_AGENT __ATOMIC_RELAXED, __HIP_MEMORY_SCOPE_AGENT

constexpr int H = 32, D = 128, PAGE = 16, NBPS = 256, SKV = NBPS * PAGE;
constexpr int NSPLIT = 32, WAVES = 4, NTHR = (WAVES + 1) * 64, NG = 8;
constexpr unsigned TOK_BYTES = H * D * 2;
constexpr unsigned PAGE_BYTES = PAGE * TOK_BYTES;
constexpr float C2 = 0.088388347648318440f * 1.4426950408889634f;
constexpr float MASK_L2 = -998244352.0f * 1.4426950408889634f;
constexpr unsigned WS_EPOCH = 0;
constexpr unsigned WS_ML = 4096;
constexpr unsigned WS_O = 32768;
constexpr unsigned WS_BYTES = WS_O + H * NSPLIT * 1024;
constexpr int AUX_NT = 2, AUX_SC1 = 16;

template <int CTRL> __device__ __forceinline__ float dpp(float x) {
  return __builtin_bit_cast(float, __builtin_amdgcn_mov_dpp(__builtin_bit_cast(int, x), CTRL, 0xf, 0xf, true));
}
template <int CTRL> __device__ __forceinline__ unsigned dppu(unsigned x) { return (unsigned)__builtin_amdgcn_mov_dpp((int)x, CTRL, 0xf, 0xf, true); }
constexpr int XOR1 = 0xB1, XOR2 = 0x4E, ROR4 = 0x124, ROR8 = 0x128, BCAST = 0x150;
__device__ __forceinline__ float fmx(float a, float b) { return __builtin_fmaxf(a, b); }
__device__ __forceinline__ float xrow_max(float x) {
  auto s = __builtin_amdgcn_permlane16_swap(__float_as_uint(x), __float_as_uint(x), false, false);
  x = fmx(__uint_as_float(s[0]), __uint_as_float(s[1]));
  auto t = __builtin_amdgcn_permlane32_swap(__float_as_uint(x), __float_as_uint(x), false, false);
  return fmx(__uint_as_float(t[0]), __uint_as_float(t[1]));
}
__device__ __forceinline__ float xrow_sum(float x) {
  auto s = __builtin_amdgcn_permlane16_swap(__float_as_uint(x), __float_as_uint(x), false, false);
  x = __uint_as_float(s[0]) + __uint_as_float(s[1]);
  auto t = __builtin_amdgcn_permlane32_swap(__float_as_uint(x), __float_as_uint(x), false, false);
  return __uint_as_float(t[0]) + __uint_as_float(t[1]);
}
__device__ __forceinline__ float dot2(unsigned k, unsigned q, float acc) {
  return __builtin_amdgcn_fdot2_f32_bf16(__builtin_bit_cast(bf16x2, k), __builtin_bit_cast(bf16x2, q), acc, false);
}
__device__ __forceinline__ unsigned cvtpk(float lo, float hi) {
  bf16x2 v = {(__bf16)lo, (__bf16)hi}; return __builtin_bit_cast(unsigned, v);
}
__device__ __forceinline__ float red4(float a, float b, float c, float d) {
  float u, w, s;
  asm("s_nop 2\n\t"
      "v_add_f32_dpp %0, %3, %3 row_ror:8 row_mask:0xf bank_mask:0x3\n\t"
      "v_add_f32_dpp %1, %4, %4 row_ror:8 row_mask:0xf bank_mask:0x3\n\t"
      "v_add_f32_dpp %0, %5, %5 row_ror:8 row_mask:0xf bank_mask:0xc\n\t"
      "v_add_f32_dpp %1, %6, %6 row_ror:8 row_mask:0xf bank_mask:0xc\n\t"
      "s_nop 1\n\t"
      "v_add_f32_dpp %2, %0, %0 row_half_mirror row_mask:0xf bank_mask:0x5\n\t"
      "v_add_f32_dpp %2, %1, %1 row_half_mirror row_mask:0xf bank_mask:0xa\n\t"
      "s_nop 1\n\t"
      "v_add_f32_dpp %2, %2, %2 quad_perm:[1,0,3,2] row_mask:0xf bank_mask:0xf\n\t"
      "s_nop 1\n\t"
      "v_add_f32_dpp %2, %2, %2 quad_perm:[2,3,0,1] row_mask:0xf bank_mask:0xf\n\t"
      "s_nop 1"
      : "=&v"(u), "=&v"(w), "=&v"(s) : "v"(a), "v"(b), "v"(c), "v"(d));
  return s;
}

#define ST(k)
#define WRST()
#define SB() __builtin_amdgcn_sched_barrier(0)

struct Args { const char* q; const char* k; const char* v; const int* bt; const int* cl; unsigned* out; char* ws; unsigned long long kv_bytes; };

__global__ void __launch_bounds__(NTHR, 2) paged_decode_kernel(Args a) {
  int ph[NG - 1], len_raw;
  {
    typedef unsigned long long u64x8 __attribute__((ext_vector_type(8)));
    u64x8 ka;
    asm volatile("s_load_dwordx16 s[36:51], %9, 0x0\n\ts_waitcnt lgkmcnt(0)\n\t"
                 "s_load_dword %0, s[42:43], %10 offset:0x0\n\ts_load_dword %7, s[44:45], 0x0\n\ts_load_dword %1, s[42:43], %10 offset:0x80\n\ts_load_dword %2, s[42:43], %10 offset:0x100\n\t"
                 "s_load_dword %3, s[42:43], %10 offset:0x180\n\ts_load_dword %4, s[42:43], %10 offset:0x200\n\ts_load_dword %5, s[42:43], %10 offset:0x280\n\ts_load_dword %6, s[42:43], %10 offset:0x300"
                 : "=&s"(ph[0]), "=&s"(ph[1]), "=&s"(ph[2]), "=&s"(ph[3]), "=&s"(ph[4]), "=&s"(ph[5]), "=&s"(ph[6]), "=&s"(len_raw), "=&{s[36:51]}"(ka)
                 : "s"(__builtin_amdgcn_kernarg_segment_ptr()), "s"((blockIdx.x & (NSPLIT - 1)) * 4));
    __builtin_amdgcn_sched_barrier(0);
    a.q = (const char*)ka[0]; a.k = (const char*)ka[1]; a.v = (const char*)ka[2]; a.bt = (const int*)ka[3]; a.cl = (const int*)ka[4];
    a.out = (unsigned*)ka[5]; a.ws = (char*)ka[6]; a.kv_bytes = ka[7];
  }
  __shared__ unsigned long long flags[2];
  const int lane = threadIdx.x & 63, wave = __builtin_amdgcn_readfirstlane(threadIdx.x >> 6);
  const int j = blockIdx.x & (NSPLIT - 1), h = (blockIdx.x >> 5) * WAVES + (wave < WAVES ? wave : (j & 3));
  gu32* epoch = (gu32*)(a.ws + WS_EPOCH + h * 128);
  const auto rw = __builtin_amdgcn_make_buffer_rsrc((void*)a.ws, 0, WS_BYTES, 0x00020000);
  asm volatile("" :: "s"(a.q), "s"(a.k), "s"(a.v), "s"(a.out), "s"(a.ws), "s"(a.kv_bytes));
  const int r = lane >> 4, c = lane & 15;
  unsigned voff = (unsigned)h * (D * 2) + (unsigned)r * TOK_BYTES + c * 16;
  const u32x4 q = *(const gv4*)(a.q + h * (D * 2) + c * 16);
  const unsigned ep = __hip_atomic_load(epoch, RLX_AGENT);
  const auto rk = __builtin_amdgcn_make_buffer_rsrc((void*)a.k, 0, (unsigned)a.kv_bytes, 0x00020000);
  unsigned vq[4] = {voff, voff + 4 * TOK_BYTES, voff + 8 * TOK_BYTES, voff + 12 * TOK_BYTES};
  asm volatile("" : "+v"(vq[0]), "+v"(vq[1]), "+v"(vq[2]), "+v"(vq[3]));
  SB();
  asm volatile("s_waitcnt lgkmcnt(0)" : "+s"(ph[0]), "+s"(ph[1]), "+s"(ph[2]), "+s"(ph[3]), "+s"(ph[4]), "+s"(ph[5]), "+s"(ph[6]), "+s"(len_raw));
  if (__builtin_expect(wave < WAVES, 1)) {
    int len = 0, nfull = 0;
    int pt[4];
    const auto rv = __builtin_amdgcn_make_buffer_rsrc((void*)a.v, 0, (unsigned)a.kv_bytes, 0x00020000);
    u32x4 buf[2 * NG][4];
    unsigned so[NG - 1], s7[4];
    auto issue = [&](int n) {
      const int t = n & (NG - 1);
      if (n < NG - 1) so[t] = len_raw > 512 * (t + 1) - PAGE ? (unsigned)ph[t] * PAGE_BYTES : 0x80000000u;
      if (n == NG - 1) {
        asm volatile("" : "+s"(pt[0]), "+s"(pt[1]), "+s"(pt[2]), "+s"(pt[3]));
#pragma unroll
        for (int b = 0; b < 4; ++b) {
          const bool live = nfull == NG || 512 * nfull + 128 * b + 16 * (j & 7) + 4 * (j >> 3) < len;
          s7[b] = live ? (unsigned)pt[b] * PAGE_BYTES + (nfull == NG ? b : j >> 3) * 4 * TOK_BYTES : 0x80000000u;
        }
      }
#pragma unroll
      for (int b = 0; b < 4; ++b)
        buf[n][b] = __builtin_amdgcn_raw_buffer_load_b128(n < NG ? rk : rv, t < NG - 1 ? vq[b] : vq[0], t < NG - 1 ? so[t] : s7[b], AUX_NT);
    };
#pragma unroll
    for (int n = 0; n < WP; ++n) { issue(n); SB(); }
    len = len_raw;
    asm volatile("" : "+s"(len));
    len = min(max(len, 0), SKV); nfull = (len + PAGE - 1) >> 9;
#pragma unroll
    for (int b = 0; b < 4; ++b) pt[b] = ((cint*)a.bt)[nfull == NG ? 32 * (NG - 1) + j : 32 * nfull + 8 * b + (j & 7)];
    __builtin_amdgcn_s_barrier();
    SB();
    ST(2);
    const int ra = len - (16 * j + (c & 12) + r), rb = len - (32 * (c & 12) + 16 * (j & 7) + 4 * (j >> 3) + r);
    const int remv = min(ra, 512 * nfull - 511);
    const bool live7 = nfull == NG ? 512 * (NG - 1) < ra : 512 * nfull < rb;
    float S[NG];
    float m = -INFINITY;
#pragma unroll
    for (int t = 0; t < NG; ++t) {
      float x[4];
#pragma unroll
      for (int i = 0; i < 4; ++i) {
        x[i] = dot2(buf[t][i][0], q[0], 0.f); x[i] = dot2(buf[t][i][1], q[1], x[i]); x[i] = dot2(buf[t][i][2], q[2], x[i]); x[i] = dot2(buf[t][i][3], q[3], x[i]);
      }
      SB();
      if (t + WP < 2 * NG) issue(t + WP);
      SB();
      const float s = red4(x[0], x[1], x[2], x[3]);
      S[t] = (t < NG - 1 ? 512 * t < remv : live7) ? s : -INFINITY;
      m = fmx(m, S[t]);
    }
    ST(4);
    m = fmx(m, dpp<ROR4>(m)); m = fmx(m, dpp<ROR8>(m)); m = xrow_max(m);
    const float mc = m * C2;
    const float msub = (m == -INFINITY) ? 0.f : -mc;
    unsigned P[NG];
    float l = 0.f;
#pragma unroll
    for (int t = 0; t < NG; ++t) {
      const float p = __builtin_amdgcn_exp2f(__builtin_fmaf(S[t], C2, msub));
      P[t] = cvtpk(p, p);
      l += __uint_as_float(P[t] & 0xffff0000u);
    }
    l += dpp<ROR4>(l); l += dpp<ROR8>(l); l = xrow_sum(l);
    const unsigned tag = ep + 1u ? ep + 1u : 1u;
    const bool sig = (j & 3) == wave && lane == 0;
    if (lane == 0) __builtin_amdgcn_raw_buffer_store_b128(u32x4{__float_as_uint(mc), tag, __float_as_uint(l), tag}, rw, 0, WS_ML + (h * NSPLIT + j) * 16, AUX_SC1);
    if (sig) __hip_atomic_store(&flags[0], (1ull << 32) | tag, __ATOMIC_RELAXED, __HIP_MEMORY_SCOPE_WORKGROUP);
    const unsigned sel = (c & 1) ? 0xffff0000u : 0x0000ffffu;
    const int dw = (c >> 1) & 3;
    const unsigned mk0 = dw == 0 ? sel : 0u, mk1 = dw == 1 ? sel : 0u, mk2 = dw == 2 ? sel : 0u, mk3 = dw == 3 ? sel : 0u;
    f32x4 acc = {0.f, 0.f, 0.f, 0.f};
    ST(5);
#pragma unroll
    for (int t = 0; t < NG; ++t) {
      const unsigned b0 = dppu<BCAST + 0>(P[t]), b1 = dppu<BCAST + 4>(P[t]), b2 = dppu<BCAST + 8>(P[t]), b3 = dppu<BCAST + 12>(P[t]);
      const unsigned bb[4] = {b0, b1, b2, b3};
#pragma unroll
      for (int i = 0; i < 4; ++i) {
        const u32x4 A = {bb[i] & mk0, bb[i] & mk1, bb[i] & mk2, bb[i] & mk3};
        acc = __builtin_amdgcn_mfma_f32_16x16x32_bf16(__builtin_bit_cast(bf16x8, A), __builtin_bit_cast(bf16x8, buf[NG + t][i]), acc, 0, 0, 0);
      }
      SB();
      if (NG + t + WP < 2 * NG) issue(NG + t + WP);
      if (t == FLAG2 && sig) __hip_atomic_store(&flags[1], 1ull, __ATOMIC_RELAXED, __HIP_MEMORY_SCOPE_WORKGROUP);
      SB();
    }
    ST(6);
    const unsigned v0 = __float_as_uint(r < 2 ? acc[0] : acc[2]), v1 = __float_as_uint(r < 2 ? acc[1] : acc[3]);
    __builtin_amdgcn_raw_buffer_store_b128(u32x4{v0, tag, v1, tag}, rw, lane * 16, WS_O + (h * NSPLIT + j) * 1024, AUX_SC1);
    ST(7);
    WRST();
    return;
  }
  {
    if (lane < 2) __hip_atomic_store(&flags[lane], 0ull, __ATOMIC_RELAXED, __HIP_MEMORY_SCOPE_WORKGROUP);
    __builtin_amdgcn_s_waitcnt(0xC07F);
    __builtin_amdgcn_s_barrier();
    {
      unsigned off = (unsigned)(lane & 31) << 21;
      off = off < (unsigned)a.kv_bytes ? off : (unsigned)a.kv_bytes - 4;
      const char* p = (lane < 32 ? a.k : a.v) + off;
      if (lane == 63) p = (const char*)a.out;
      if (PF_TLB == 2) p = (const char*)a.out;
      const unsigned pf1 = *(const gu32*)p;
      asm volatile("" :: "v"(pf1));
    }
    const int len = len_raw < 0 ? 0 : (len_raw > SKV ? SKV : len_raw);
    const int dp = 4 * (8 * ((j >> 2) & 1) + (lane & 7)) + 2 * ((j >> 3) & 1) + (j >> 4);
    ST(1); ST(2);
    unsigned long long f0;
    for (;;) {
      f0 = __hip_atomic_load(&flags[0], __ATOMIC_RELAXED, __HIP_MEMORY_SCOPE_WORKGROUP);
      if (f0 >> 32) break;
      __builtin_amdgcn_s_sleep(8);
    }
    const unsigned tag = (unsigned)f0;
    ST(3);
    u32x4 ml[4];
    unsigned vo[4], o0[4] = {0u, 0u, 0u, 0u}, o1[4] = {0u, 0u, 0u, 0u}, miss;
#pragma unroll
    for (int k = 0; k < 4; ++k) vo[k] = (k * 8 + (lane >> 3)) * 1024 + (lane & 7) * 16;
    const unsigned so = WS_O + h * NSPLIT * 1024 + (j >> 2) * 128;
    auto take = [&](const u32x4* g) {
      miss = 0u;
#pragma unroll
      for (int k = 0; k < 4; ++k) {
        const bool o = g[k][1] == tag && g[k][3] == tag;
        o0[k] = o ? g[k][0] : o0[k]; o1[k] = o ? g[k][2] : o1[k];
        vo[k] = o ? 0x80000000u : vo[k];
        miss += (~vo[k]) >> 31;
      }
    };
    while (__hip_atomic_load(&flags[1], __ATOMIC_RELAXED, __HIP_MEMORY_SCOPE_WORKGROUP) == 0ull) __builtin_amdgcn_s_sleep(2);
    for (unsigned spins = 0u; spins < (1u << 22); ++spins) {
      bool o = true;
#pragma unroll
      for (int k = 0; k < 4; ++k) ml[k] = __builtin_amdgcn_raw_buffer_load_b128(rw, (lane >> 3) * 16 + k * 128, WS_ML + h * NSPLIT * 16, AUX_SC1);
#pragma unroll
      for (int k = 0; k < 4; ++k) o = o & (ml[k][1] == tag) & (ml[k][3] == tag);
      asm volatile("" ::: "memory");
      if (__all(o)) break;
    }
    ST(4);
    if (j < 4 && lane == 0) __hip_atomic_store(epoch, tag, RLX_AGENT);
    u32x4 srd = {(unsigned)(size_t)a.ws, (unsigned)((size_t)a.ws >> 32) & 0xffffu, WS_BYTES, 0x00020000u};
    u32x4 A[4], B[4];
#define PLD(X, S) do { _Pragma("unroll") for (int k = 0; k < 4; ++k) asm volatile("buffer_load_dwordx4 %0, %1, %2, %3 offen sc1" : "=v"(X[k]) : "v"(vo[k]), "s"(srd), "s"(S) : "memory"); } while (0)
#define PWAIT(X) asm volatile("s_waitcnt vmcnt(4)" : "+v"(X[0]), "+v"(X[1]), "+v"(X[2]), "+v"(X[3]) :: "memory")
    PLD(A, so);
    float wn[4], e0 = 0.f, e1 = 0.f;
    {
      float Mx = fmx(fmx(__uint_as_float(ml[0][0]), __uint_as_float(ml[1][0])), fmx(__uint_as_float(ml[2][0]), __uint_as_float(ml[3][0])));
      Mx = fmx(Mx, dpp<ROR8>(Mx)); Mx = xrow_max(Mx);
      const bool slow = (len < SKV) && !(MASK_L2 - Mx < -160.f);
      if (slow) Mx = fmx(Mx, MASK_L2);
      const float ms2 = (Mx == -INFINITY) ? 0.f : Mx;
      float y = 0.f;
#pragma unroll
      for (int k = 0; k < 4; ++k) { wn[k] = __builtin_amdgcn_exp2f(__uint_as_float(ml[k][0]) - ms2); y = __builtin_fmaf(wn[k], __uint_as_float(ml[k][2]), y); }
      if (__builtin_expect(slow, 0)) {
        const float wm = __builtin_amdgcn_exp2f(MASK_L2 - ms2);
        float cnt = 0.f;
        for (int t = len + (lane >> 3); t < SKV; t += 8) {
          const unsigned vv = *(const gu32*)(a.v + (size_t)((const gu32*)a.bt)[t >> 4] * PAGE_BYTES + (size_t)(t & 15) * TOK_BYTES + h * (D * 2) + dp * 4);
          e0 += __uint_as_float(vv << 16); e1 += __uint_as_float(vv & 0xffff0000u); cnt += 1.f;
        }
        e0 *= wm; e1 *= wm; y += wm * cnt;
      }
      y += dpp<ROR8>(y); y = xrow_sum(y);
      const float ry = __builtin_amdgcn_rcpf(y);
#pragma unroll
      for (int k = 0; k < 4; ++k) wn[k] *= ry;
      e0 *= ry; e1 *= ry;
      asm volatile("" : "+v"(wn[0]), "+v"(wn[1]), "+v"(wn[2]), "+v"(wn[3]), "+v"(e0), "+v"(e1));
    }
    ST(5);
    ST(6);
    unsigned sb = 0x80000000u;
    for (unsigned spins = 0u; spins < (1u << 21); ++spins) {
      PLD(B, sb);
      PWAIT(A);
      take(A);
      unsigned long long mm = __ballot(miss != 0u);
      if (mm == 0ull) break;
      PLD(A, so);
      PWAIT(B);
      take(B);
      mm = __ballot(miss != 0u);
      if (mm == 0ull) break;
      if (sb != so && __builtin_popcountll(mm) + __builtin_popcountll(__ballot(miss > 1u)) * 2 <= TH * 8) { sb = so; __builtin_amdgcn_s_sleep(SLP); }
    }
    float x0 = e0, x1 = e1;
#pragma unroll
    for (int k = 0; k < 4; ++k) { x0 = __builtin_fmaf(wn[k], __uint_as_float(o0[k]), x0); x1 = __builtin_fmaf(wn[k], __uint_as_float(o1[k]), x1); }
    ST(7);
    x0 += dpp<ROR8>(x0); x1 += dpp<ROR8>(x1);
    x0 = xrow_sum(x0); x1 = xrow_sum(x1);
    if (lane < 8) ((gu32*)a.out)[h * (D / 2) + dp] = cvtpk(x0, x1);
    asm volatile("s_waitcnt vmcnt(0)" :: "v"(A[0]), "v"(A[1]), "v"(A[2]), "v"(A[3]), "v"(B[0]), "v"(B[1]), "v"(B[2]), "v"(B[3]));
  }
  WRST();
}

extern "C" void kernel_launch(void* const* d_in, const int* in_sizes, int n_in, void* d_out, int out_size, void* d_ws, size_t ws_size,
                              hipStream_t stream) {
  Args a{(const char*)d_in[0], (const char*)d_in[1], (const char*)d_in[2], (const int*)d_in[3], (const int*)d_in[4],
         (unsigned*)d_out, (char*)d_ws, (unsigned long long)in_sizes[1] * 2ull};
  paged_decode_kernel<<<dim3(H * NSPLIT / WAVES), dim3(NTHR), 0, stream>>>(a);
}
```

```cpp
#include <hip/hip_runtime.h>
#include <hip/hip_bf16.h>
#include <stdint.h>

typedef unsigned u32x4 __attribute__((ext_vector_type(4)));
typedef float f32x4 __attribute__((ext_vector_type(4)));
typedef __bf16 bf16x2 __attribute__((ext_vector_type(2)));
typedef __bf16 bf16x8 __attribute__((ext_vector_type(8)));
#define GAS __attribute__((address_space(1)))
typedef GAS unsigned gu32;
typedef GAS u32x4 gv4;
typedef __attribute__((address_space(4))) const int cint;
#define RLX_AGENT __ATOMIC_RELAXED, __HIP_MEMORY_SCOPE_AGENT

constexpr int H = 32, D = 128, PAGE = 16, NBPS = 256, SKV = NBPS * PAGE;
constexpr int NSPLIT = 32, WAVES = 4, NTHR = (WAVES + 1) * 64, NG = 8;
constexpr int WP = 4;
constexpr int FLAG2 = 2;
constexpr unsigned TOK_BYTES = H * D * 2;
constexpr unsigned PAGE_BYTES = PAGE * TOK_BYTES;
constexpr float C2 = 0.088388347648318440f * 1.4426950408889634f;
constexpr float MASK_L2 = -998244352.0f * 1.4426950408889634f;
constexpr unsigned WS_EPOCH = 0;
constexpr unsigned WS_ML = 4096;
constexpr unsigned WS_O = 32768;
constexpr unsigned WS_BYTES = WS_O + H * NSPLIT * 1024;
constexpr int AUX_NT = 2, AUX_SC1 = 16;

template <int CTRL> __device__ __forceinline__ float dpp(float x) {
  return __builtin_bit_cast(float, __builtin_amdgcn_mov_dpp(__builtin_bit_cast(int, x), CTRL, 0xf, 0xf, true));
}
template <int CTRL> __device__ __forceinline__ unsigned dppu(unsigned x) { return (unsigned)__builtin_amdgcn_mov_dpp((int)x, CTRL, 0xf, 0xf, true); }
constexpr int XOR1 = 0xB1, XOR2 = 0x4E, ROR4 = 0x124, ROR8 = 0x128, BCAST = 0x150;
__device__ __forceinline__ float fmx(float a, float b) { return __builtin_fmaxf(a, b); }
__device__ __forceinline__ float xrow_max(float x) {
  auto s = __builtin_amdgcn_permlane16_swap(__float_as_uint(x), __float_as_uint(x), false, false);
  x = fmx(__uint_as_float(s[0]), __uint_as_float(s[1]));
  auto t = __builtin_amdgcn_permlane32_swap(__float_as_uint(x), __float_as_uint(x), false, false);
  return fmx(__uint_as_float(t[0]), __uint_as_float(t[1]));
}
__device__ __forceinline__ float xrow_sum(float x) {
  auto s = __builtin_amdgcn_permlane16_swap(__float_as_uint(x), __float_as_uint(x), false, false);
  x = __uint_as_float(s[0]) + __uint_as_float(s[1]);
  auto t = __builtin_amdgcn_permlane32_swap(__float_as_uint(x), __float_as_uint(x), false, false);
  return __uint_as_float(t[0]) + __uint_as_float(t[1]);
}
__device__ __forceinline__ float dot2(unsigned k, unsigned q, float acc) {
  return __builtin_amdgcn_fdot2_f32_bf16(__builtin_bit_cast(bf16x2, k), __builtin_bit_cast(bf16x2, q), acc, false);
}
__device__ __forceinline__ unsigned cvtpk(float lo, float hi) {
  bf16x2 v = {(__bf16)lo, (__bf16)hi}; return __builtin_bit_cast(unsigned, v);
}
__device__ __forceinline__ float red4(float a, float b, float c, float d) {
  float u, w, s;
  asm("s_nop 2\n\t"
      "v_add_f32_dpp %0, %3, %3 row_ror:8 row_mask:0xf bank_mask:0x3\n\t"
      "v_add_f32_dpp %1, %4, %4 row_ror:8 row_mask:0xf bank_mask:0x3\n\t"
      "v_add_f32_dpp %0, %5, %5 row_ror:8 row_mask:0xf bank_mask:0xc\n\t"
      "v_add_f32_dpp %1, %6, %6 row_ror:8 row_mask:0xf bank_mask:0xc\n\t"
      "s_nop 1\n\t"
      "v_add_f32_dpp %2, %0, %0 row_half_mirror row_mask:0xf bank_mask:0x5\n\t"
      "v_add_f32_dpp %2, %1, %1 row_half_mirror row_mask:0xf bank_mask:0xa\n\t"
      "s_nop 1\n\t"
      "v_add_f32_dpp %2, %2, %2 quad_perm:[1,0,3,2] row_mask:0xf bank_mask:0xf\n\t"
      "s_nop 1\n\t"
      "v_add_f32_dpp %2, %2, %2 quad_perm:[2,3,0,1] row_mask:0xf bank_mask:0xf\n\t"
      "s_nop 1"
      : "=&v"(u), "=&v"(w), "=&v"(s) : "v"(a), "v"(b), "v"(c), "v"(d));
  return s;
}

#define SB() __builtin_amdgcn_sched_barrier(0)

struct Args { const char* q; const char* k; const char* v; const int* bt; const int* cl; unsigned* out; char* ws; unsigned kv_bytes; unsigned step; };

__global__ void __launch_bounds__(NTHR, 2) paged_decode_kernel(Args a) {
  __shared__ __attribute__((aligned(16))) unsigned long long flags[2];
  int ph[NG - 1], len_raw;
  {
    typedef unsigned long long u64x8 __attribute__((ext_vector_type(8)));
    u64x8 ka;
    asm volatile("s_load_dwordx16 s[36:51], %1, 0x0" : "=&{s[36:51]}"(ka) : "s"(__builtin_amdgcn_kernarg_segment_ptr()));
    __builtin_amdgcn_sched_barrier(0);
    *(volatile __attribute__((address_space(3))) u32x4*)flags = u32x4{0u, 0u, 0u, 0u};
    __builtin_amdgcn_sched_barrier(0);
    unsigned tk, tv;
    asm volatile("s_waitcnt lgkmcnt(0)\n\t"
                 "s_load_dword %0, s[42:43], %11 offset:0x0\n\ts_load_dword %7, s[44:45], 0x0\n\ts_load_dword %1, s[42:43], %11 offset:0x80\n\ts_load_dword %2, s[42:43], %11 offset:0x100\n\t"
                 "s_load_dword %3, s[42:43], %11 offset:0x180\n\ts_load_dword %4, s[42:43], %11 offset:0x200\n\ts_load_dword %5, s[42:43], %11 offset:0x280\n\ts_load_dword %6, s[42:43], %11 offset:0x300\n\t"
                 "s_cmpk_lt_u32 %12, 0x100\n\ts_cbranch_scc1 1f\n\t"
                 "s_mul_i32 %10, %13, s51\n\ts_load_dword %9, s[38:39], %10\n\ts_load_dword %9, s[38:39], %10 offset:0x80000\n\ts_waitcnt lgkmcnt(0)\n\t"
                 "s_load_dword %9, s[40:41], %10 offset:0x80000\n\ts_load_dword %10, s[40:41], %10\n\t"
                 "s_barrier\n\ts_waitcnt lgkmcnt(0)\n\t1:"
                 : "=&s"(ph[0]), "=&s"(ph[1]), "=&s"(ph[2]), "=&s"(ph[3]), "=&s"(ph[4]), "=&s"(ph[5]), "=&s"(ph[6]), "=&s"(len_raw), "+{s[36:51]}"(ka), "=&s"(tk), "=&s"(tv)
                 : "s"((blockIdx.x & (NSPLIT - 1)) * 4), "s"(__builtin_amdgcn_readfirstlane(threadIdx.x)), "s"(blockIdx.x >> 3) : "memory", "scc");
    __builtin_amdgcn_sched_barrier(0);
    a.q = (const char*)ka[0]; a.k = (const char*)ka[1]; a.v = (const char*)ka[2]; a.bt = (const int*)ka[3]; a.cl = (const int*)ka[4];
    a.out = (unsigned*)ka[5]; a.ws = (char*)ka[6]; a.kv_bytes = (unsigned)ka[7];
  }
  const int lane = threadIdx.x & 63, wave = __builtin_amdgcn_readfirstlane(threadIdx.x >> 6);
  const int j = blockIdx.x & (NSPLIT - 1), h = (blockIdx.x >> 5) * WAVES + (wave < WAVES ? wave : (j & 3));
  gu32* epoch = (gu32*)(a.ws + WS_EPOCH + h * 128);
  const auto rw = __builtin_amdgcn_make_buffer_rsrc((void*)a.ws, 0, WS_BYTES, 0x00020000);
  asm volatile("" :: "s"(a.q), "s"(a.k), "s"(a.v), "s"(a.out), "s"(a.ws), "s"(a.kv_bytes));
  const int r = lane >> 4, c = lane & 15;
  unsigned voff = (unsigned)h * (D * 2) + (unsigned)r * TOK_BYTES + c * 16;
  const u32x4 q = *(const gv4*)(a.q + h * (D * 2) + c * 16);
  const auto rk = __builtin_amdgcn_make_buffer_rsrc((void*)a.k, 0, a.kv_bytes, 0x00020000);
  unsigned vq[4] = {voff, voff + 4 * TOK_BYTES, voff + 8 * TOK_BYTES, voff + 12 * TOK_BYTES};
  asm volatile("" : "+v"(vq[0]), "+v"(vq[1]), "+v"(vq[2]), "+v"(vq[3]));
  SB();
  asm volatile("s_waitcnt lgkmcnt(0)" : "+s"(ph[0]), "+s"(ph[1]), "+s"(ph[2]), "+s"(ph[3]), "+s"(ph[4]), "+s"(ph[5]), "+s"(ph[6]), "+s"(len_raw));
  if (__builtin_expect(wave < WAVES, 1)) {
    int len = 0, nfull = 0;
    int pt[4];
    const auto rv = __builtin_amdgcn_make_buffer_rsrc((void*)a.v, 0, a.kv_bytes, 0x00020000);
    u32x4 buf[2 * NG][4];
    unsigned so[NG - 1], s7[4];
    auto issue = [&](int n) {
      const int t = n & (NG - 1);
      if (n < NG - 1) so[t] = len_raw > 512 * (t + 1) - PAGE ? (unsigned)ph[t] * PAGE_BYTES : 0x80000000u;
      if (n == NG - 1) {
        asm volatile("" : "+s"(pt[0]), "+s"(pt[1]), "+s"(pt[2]), "+s"(pt[3]));
#pragma unroll
        for (int b = 0; b < 4; ++b) {
          const bool live = nfull == NG || 512 * nfull + 128 * b + 16 * (j & 7) + 4 * (j >> 3) < len;
          s7[b] = live ? (unsigned)pt[b] * PAGE_BYTES + (nfull == NG ? b : j >> 3) * 4 * TOK_BYTES : 0x80000000u;
        }
      }
#pragma unroll
      for (int b = 0; b < 4; ++b)
        buf[n][b] = __builtin_amdgcn_raw_buffer_load_b128(n < NG ? rk : rv, t < NG - 1 ? vq[b] : vq[0], t < NG - 1 ? so[t] : s7[b], AUX_NT);
    };
#pragma unroll
    for (int n = 0; n < 2; ++n) { issue(n); SB(); }
    len = len_raw;
    asm volatile("" : "+s"(len));
    len = min(max(len, 0), SKV); nfull = (len + PAGE - 1) >> 9;
#pragma unroll
    for (int b = 0; b < 4; ++b) pt[b] = ((cint*)a.bt)[nfull == NG ? 32 * (NG - 1) + j : 32 * nfull + 8 * b + (j & 7)];
    SB();
#pragma unroll
    for (int n = 2; n < WP; ++n) { issue(n); SB(); }
    const unsigned ep = __hip_atomic_load(epoch, RLX_AGENT);
    SB();
    __builtin_amdgcn_s_barrier();
    SB();
    const int ra = len - (16 * j + (c & 12) + r), rb = len - (32 * (c & 12) + 16 * (j & 7) + 4 * (j >> 3) + r);
    const int remv = min(ra, 512 * nfull - 511);
    const bool live7 = nfull == NG ? 512 * (NG - 1) < ra : 512 * nfull < rb;
    float S[NG];
    float m = -INFINITY;
#pragma unroll
    for (int t = 0; t < NG; ++t) {
      float x[4];
#pragma unroll
      for (int i = 0; i < 4; ++i) {
        x[i] = dot2(buf[t][i][0], q[0], 0.f); x[i] = dot2(buf[t][i][1], q[1], x[i]); x[i] = dot2(buf[t][i][2], q[2], x[i]); x[i] = dot2(buf[t][i][3], q[3], x[i]);
      }
      SB();
      if (t + WP < 2 * NG) issue(t + WP);
      SB();
      const float s = red4(x[0], x[1], x[2], x[3]);
      S[t] = (t < NG - 1 ? 512 * t < remv : live7) ? s : -INFINITY;
      m = fmx(m, S[t]);
    }
    m = fmx(m, dpp<ROR4>(m)); m = fmx(m, dpp<ROR8>(m)); m = xrow_max(m);
    const float mc = m * C2;
    const float msub = (m == -INFINITY) ? 0.f : -mc;
    unsigned P[NG];
    float l = 0.f;
#pragma unroll
    for (int t = 0; t < NG; ++t) {
      const float p = __builtin_amdgcn_exp2f(__builtin_fmaf(S[t], C2, msub));
      P[t] = cvtpk(p, p);
      l += __uint_as_float(P[t] & 0xffff0000u);
    }
    l += dpp<ROR4>(l); l += dpp<ROR8>(l); l = xrow_sum(l);
    const unsigned tag = ep + 1u ? ep + 1u : 1u;
    const bool sig = (j & 3) == wave && lane == 0;
    if (lane == 0) __builtin_amdgcn_raw_buffer_store_b128(u32x4{__float_as_uint(mc), tag, __float_as_uint(l), tag}, rw, 0, WS_ML + (h * NSPLIT + j) * 16, AUX_SC1);
    if (sig) __hip_atomic_store(&flags[0], (1ull << 32) | tag, __ATOMIC_RELAXED, __HIP_MEMORY_SCOPE_WORKGROUP);
    const unsigned sel = (c & 1) ? 0xffff0000u : 0x0000ffffu;
    const int dw = (c >> 1) & 3;
    const unsigned mk0 = dw == 0 ? sel : 0u, mk1 = dw == 1 ? sel : 0u, mk2 = dw == 2 ? sel : 0u, mk3 = dw == 3 ? sel : 0u;
    f32x4 acc = {0.f, 0.f, 0.f, 0.f};
#pragma unroll
    for (int t = 0; t < NG; ++t) {
      const unsigned b0 = dppu<BCAST + 0>(P[t]), b1 = dppu<BCAST + 4>(P[t]), b2 = dppu<BCAST + 8>(P[t]), b3 = dppu<BCAST + 12>(P[t]);
      const unsigned bb[4] = {b0, b1, b2, b3};
#pragma unroll
      for (int i = 0; i < 4; ++i) {
        const u32x4 A = {bb[i] & mk0, bb[i] & mk1, bb[i] & mk2, bb[i] & mk3};
        acc = __builtin_amdgcn_mfma_f32_16x16x32_bf16(__builtin_bit_cast(bf16x8, A), __builtin_bit_cast(bf16x8, buf[NG + t][i]), acc, 0, 0, 0);
      }
      SB();
      if (NG + t + WP < 2 * NG) issue(NG + t + WP);
      if (t == FLAG2 && sig) __hip_atomic_store(&flags[1], 1ull, __ATOMIC_RELAXED, __HIP_MEMORY_SCOPE_WORKGROUP);
      SB();
    }
    const unsigned v0 = __float_as_uint(r < 2 ? acc[0] : acc[2]), v1 = __float_as_uint(r < 2 ? acc[1] : acc[3]);
    __builtin_amdgcn_raw_buffer_store_b128(u32x4{v0, tag, v1, tag}, rw, lane * 16, WS_O + (h * NSPLIT + j) * 1024, AUX_SC1);
    return;
  }
  {
    {
      const unsigned pf = *(const gu32*)a.out;
      asm volatile("" :: "v"(pf));
    }
    const int len = len_raw < 0 ? 0 : (len_raw > SKV ? SKV : len_raw);
    const int dp = 4 * (8 * ((j >> 2) & 1) + (lane & 7)) + 2 * ((j >> 3) & 1) + (j >> 4);
    unsigned long long f0;
    for (;;) {
      f0 = __hip_atomic_load(&flags[0], __ATOMIC_RELAXED, __HIP_MEMORY_SCOPE_WORKGROUP);
      if (f0 >> 32) break;
      __builtin_amdgcn_s_sleep(8);
    }
    const unsigned tag = (unsigned)f0;
    u32x4 ml[4];
    unsigned vo[4], o0[4] = {0u, 0u, 0u, 0u}, o1[4] = {0u, 0u, 0u, 0u}, miss;
#pragma unroll
    for (int k = 0; k < 4; ++k) vo[k] = (k * 8 + (lane >> 3)) * 1024 + (lane & 7) * 16;
    const unsigned so = WS_O + h * NSPLIT * 1024 + (j >> 2) * 128;
    auto take = [&](const u32x4* g) {
      miss = 0u;
#pragma unroll
      for (int k = 0; k < 4; ++k) {
        const bool o = g[k][1] == tag && g[k][3] == tag;
        o0[k] = o ? g[k][0] : o0[k]; o1[k] = o ? g[k][2] : o1[k];
        vo[k] = o ? 0x80000000u : vo[k];
        miss += (~vo[k]) >> 31;
      }
    };
    while (__hip_atomic_load(&flags[1], __ATOMIC_RELAXED, __HIP_MEMORY_SCOPE_WORKGROUP) == 0ull) __builtin_amdgcn_s_sleep(2);
    for (unsigned spins = 0u; spins < (1u << 22); ++spins) {
      bool o = true;
#pragma unroll
      for (int k = 0; k < 4; ++k) ml[k] = __builtin_amdgcn_raw_buffer_load_b128(rw, (lane >> 3) * 16 + k * 128, WS_ML + h * NSPLIT * 16, AUX_SC1);
#pragma unroll
      for (int k = 0; k < 4; ++k) o = o & (ml[k][1] == tag) & (ml[k][3] == tag);
      asm volatile("" ::: "memory");
      if (__all(o)) break;
    }
    if (j < 4 && lane == 0) __hip_atomic_store(epoch, tag, RLX_AGENT);
    u32x4 srd = {(unsigned)(size_t)a.ws, (unsigned)((size_t)a.ws >> 32) & 0xffffu, WS_BYTES, 0x00020000u};
    u32x4 A[4], B[4];
#define PLD(X, S) do { _Pragma("unroll") for (int k = 0; k < 4; ++k) asm volatile("buffer_load_dwordx4 %0, %1, %2, %3 offen sc1" : "=v"(X[k]) : "v"(vo[k]), "s"(srd), "s"(S) : "memory"); } while (0)
#define PWAIT(X) asm volatile("s_waitcnt vmcnt(4)" : "+v"(X[0]), "+v"(X[1]), "+v"(X[2]), "+v"(X[3]) :: "memory")
    PLD(A, so);
    float wn[4], e0 = 0.f, e1 = 0.f;
    {
      float Mx = fmx(fmx(__uint_as_float(ml[0][0]), __uint_as_float(ml[1][0])), fmx(__uint_as_float(ml[2][0]), __uint_as_float(ml[3][0])));
      Mx = fmx(Mx, dpp<ROR8>(Mx)); Mx = xrow_max(Mx);
      const bool slow = (len < SKV) && !(MASK_L2 - Mx < -160.f);
      if (slow) Mx = fmx(Mx, MASK_L2);
      const float ms2 = (Mx == -INFINITY) ? 0.f : Mx;
      float y = 0.f;
#pragma unroll
      for (int k = 0; k < 4; ++k) { wn[k] = __builtin_amdgcn_exp2f(__uint_as_float(ml[k][0]) - ms2); y = __builtin_fmaf(wn[k], __uint_as_float(ml[k][2]), y); }
      if (__builtin_expect(slow, 0)) {
        const float wm = __builtin_amdgcn_exp2f(MASK_L2 - ms2);
        float cnt = 0.f;
        for (int t = len + (lane >> 3); t < SKV; t += 8) {
          const unsigned vv = *(const gu32*)(a.v + (size_t)((const gu32*)a.bt)[t >> 4] * PAGE_BYTES + (size_t)(t & 15) * TOK_BYTES + h * (D * 2) + dp * 4);
          e0 += __uint_as_float(vv << 16); e1 += __uint_as_float(vv & 0xffff0000u); cnt += 1.f;
        }
        e0 *= wm; e1 *= wm; y += wm * cnt;
      }
      y += dpp<ROR8>(y); y = xrow_sum(y);
      const float ry = __builtin_amdgcn_rcpf(y);
#pragma unroll
      for (int k = 0; k < 4; ++k) wn[k] *= ry;
      e0 *= ry; e1 *= ry;
      asm volatile("" : "+v"(wn[0]), "+v"(wn[1]), "+v"(wn[2]), "+v"(wn[3]), "+v"(e0), "+v"(e1));
    }
    unsigned sb = 0x80000000u;
    for (unsigned spins = 0u; spins < (1u << 21); ++spins) {
      PLD(B, sb);
      PWAIT(A);
      take(A);
      unsigned long long mm = __ballot(miss != 0u);
      if (mm == 0ull) break;
      PLD(A, so);
      PWAIT(B);
      take(B);
      mm = __ballot(miss != 0u);
      if (mm == 0ull) break;
      if (sb != so && __builtin_popcountll(mm) + __builtin_popcountll(__ballot(miss > 1u)) * 2 <= 64) { sb = so; __builtin_amdgcn_s_sleep(8); }
    }
    float x0 = e0, x1 = e1;
#pragma unroll
    for (int k = 0; k < 4; ++k) { x0 = __builtin_fmaf(wn[k], __uint_as_float(o0[k]), x0); x1 = __builtin_fmaf(wn[k], __uint_as_float(o1[k]), x1); }
    x0 += dpp<ROR8>(x0); x1 += dpp<ROR8>(x1);
    x0 = xrow_sum(x0); x1 = xrow_sum(x1);
    if (lane < 8) ((gu32*)a.out)[h * (D / 2) + dp] = cvtpk(x0, x1);
    asm volatile("s_waitcnt vmcnt(0)" :: "v"(A[0]), "v"(A[1]), "v"(A[2]), "v"(A[3]), "v"(B[0]), "v"(B[1]), "v"(B[2]), "v"(B[3]));
  }
}

extern "C" void kernel_launch(void* const* d_in, const int* in_sizes, int n_in, void* d_out, int out_size, void* d_ws, size_t ws_size,
                              hipStream_t stream) {
  const unsigned kvb = (unsigned)in_sizes[1] * 2u;
  Args a{(const char*)d_in[0], (const char*)d_in[1], (const char*)d_in[2], (const int*)d_in[3], (const int*)d_in[4],
         (unsigned*)d_out, (char*)d_ws, kvb, ((kvb - 16u - 0x80000u) / 31u) & ~15u};
  paged_decode_kernel<<<dim3(H * NSPLIT / WAVES), dim3(NTHR), 0, stream>>>(a);
}
```
